```python
import torch

def _softmax(x, axis=-1):
    return torch.softmax(x, dim=axis)
def _gelu(x):
    return torch.nn.functional.gelu(x, approximate="tanh")
def _silu(x):
    return torch.nn.functional.silu(x)
def _sigmoid(x):
    return torch.sigmoid(x)
def _layernorm(x, axis=-1, eps=1e-5):
    m = x.mean(dim=axis, keepdim=True)
    v = x.var(dim=axis, keepdim=True, unbiased=False)
    return (x - m) / torch.sqrt(v + eps)
def _scatter_add(tgt, idx, src, axis=0):
    return tgt.clone().index_add_(axis, idx, src)
def _segment_sum(src, seg, n):
    out = torch.zeros((n,) + src.shape[1:], dtype=src.dtype)
    return out.index_add_(0, seg, src)
def _pad(x, pw):
    flat = tuple(v for pair in reversed(pw) for v in pair)
    return torch.nn.functional.pad(x, flat)

kernel_name = 'sequential_preLN__gqa_block_cached__glu_gated_glu__decode'

def setup_inputs():

    _w = (lambda a: (lambda t: t.to(torch.bfloat16) if t.is_floating_point() else t)(torch.as_tensor(a)))
    def _A_setup():
        B, Sq, D = 1, 1, 512
        Hq, Hkv, Sk, Dh = 32, 8, 65536, 128
        assert Hq % Hkv == 0, (Hq, Hkv)
        return {
            "x": _w((lambda *s: torch.randn(*s, dtype=torch.bfloat16) * 0.05)(B, Sq, D)),
            "k_cache": _w((lambda *s: torch.randn(*s, dtype=torch.bfloat16) * 3.2)(B, Hkv, Sk, Dh)),
            "v_cache": _w((lambda *s: torch.randn(*s, dtype=torch.bfloat16) * 0.4)(B, Hkv, Sk, Dh)),
            "wq": _w((lambda *s: torch.randn(*s, dtype=torch.bfloat16) * 3.2)(D, Hq * Dh) * (D ** -0.5)),
            "wo": _w((lambda *s: torch.randn(*s, dtype=torch.bfloat16) * 0.4)(Hq * Dh, D) * ((Hq * Dh) ** -0.5)),
        }
    _A_in = _A_setup()

    def _B_setup():
        B, S, D, F = 1, 1, 512, 1792
        return {
            "x": _w((lambda *s: torch.randn(*s, dtype=torch.bfloat16) * 0.05)(B, S, D)),
            "w_gate": _w((lambda *s: torch.randn(*s, dtype=torch.bfloat16) * 0.2)(D, F)),
            "w_up": _w((lambda *s: torch.randn(*s, dtype=torch.bfloat16) * 0.05)(D, F)),
            "w_down": _w((lambda *s: torch.randn(*s, dtype=torch.bfloat16) * 0.05)(F, D)),
        }
    _B_in = _B_setup()

    return {
        "A_x": _A_in["x"],
        "A_k_cache": _A_in["k_cache"],
        "A_v_cache": _A_in["v_cache"],
        "A_wq": _A_in["wq"],
        "A_wo": _A_in["wo"],
        "B_x": _B_in["x"],
        "B_w_gate": _B_in["w_gate"],
        "B_w_up": _B_in["w_up"],
        "B_w_down": _B_in["w_down"],
    }

def reference(A_x, A_k_cache, A_v_cache, A_wq, A_wo, B_x, B_w_gate, B_w_up, B_w_down):
    def _A_ref(x, k_cache, v_cache, wq, wo):
        B, Sq, D = x.shape
        Hq, Dh = 32, 128
        Hkv = k_cache.shape[1]
        rep = Hq // Hkv
        q = (lambda x, *d: x.permute(*d))((x @ wq).reshape(B, Sq, Hq, Dh), 0, 2, 1, 3)
        k = (lambda x, n, axis: torch.repeat_interleave(x, n, dim=axis))(k_cache, rep, axis=1)
        v = (lambda x, n, axis: torch.repeat_interleave(x, n, dim=axis))(v_cache, rep, axis=1)
        scores = torch.einsum("bhqd,bhkd->bhqk", q, k) * (Dh ** -0.5)
        p = _softmax(scores, axis=-1)
        o = torch.einsum("bhqk,bhkd->bhqd", p, v)
        return (lambda x, *d: x.permute(*d))(o, 0, 2, 1, 3).reshape(B, Sq, Hq * Dh) @ wo

    def _B_ref(x, w_gate, w_up, w_down):
        g = torch.einsum("bsd,df->bsf", x, w_gate)
        u = torch.einsum("bsd,df->bsf", x, w_up)
        h = _sigmoid(g) * u
        return torch.einsum("bsf,fd->bsd", h, w_down)

    x = A_x
    h = x + _A_ref(_layernorm(x, axis=-1), A_k_cache, A_v_cache, A_wq, A_wo)
    h2 = h + _B_ref(_layernorm(h, axis=-1), B_w_gate, B_w_up, B_w_down)
    return h2
```

```cpp
#include <hip/hip_runtime.h>
#include <stdint.h>

#define DIAG 0
#define PRIO 1
#define SH_Q 120
#define SH_G 122
#define SH_X 130
#define SH_2 134
#define SH_3 135
#define SH_PAR 0
#define WAUX 0

typedef unsigned v4u __attribute__((ext_vector_type(4)));
typedef unsigned v2u __attribute__((ext_vector_type(2)));
typedef float f32x4 __attribute__((ext_vector_type(4)));
typedef __bf16 bf16x8 __attribute__((ext_vector_type(8)));
typedef unsigned short u16;
#define GAS __attribute__((address_space(1)))
#define LAS __attribute__((address_space(3)))
#define RLX_AGENT __ATOMIC_RELAXED, __HIP_MEMORY_SCOPE_AGENT

constexpr int D = 512, SK = 65536, FF = 1792;
constexpr int TB = 4;
constexpr unsigned WS_EPOCH = 0;
constexpr unsigned WS_QG = 4096;
constexpr unsigned WS_ML = WS_QG + 16384;
constexpr unsigned WS_HG = WS_ML + 16384;
constexpr unsigned WS_PO = WS_HG + 4096;
constexpr unsigned WS_HP = WS_PO + 1048576;
constexpr unsigned WS_OP = WS_HP + 1048576;
constexpr unsigned WS_END = WS_OP + 262144;
constexpr unsigned WS_STAMP = 4u << 20;
constexpr unsigned SPIN_CAP = 1u << 18;
constexpr int AUX_SC1 = 16, AUX_NT = 2;
constexpr int RSRC_W3 = 0x00020000;
constexpr int OOB = 0x40000000;
constexpr int cmin(int a, int b) { return a < b ? a : b; }
constexpr int cmax(int a, int b) { return a > b ? a : b; }
constexpr int SH_ABS = SH_PAR < 0 ? -SH_PAR : SH_PAR;
constexpr int NBMIN = (cmin(cmin(cmin(SH_Q, SH_G), cmin(SH_X, SH_2)), SH_3) - SH_ABS) >> 3;
constexpr int NBMAX = (cmax(cmax(cmax(SH_Q, SH_G), cmax(SH_X, SH_2)), SH_3) + SH_ABS + 7) >> 3;
static_assert(8 * SH_Q + 7 * SH_G + SH_X + 8 * SH_2 + 8 * SH_3 == 4096, "shares must cover all blocks");

__device__ __forceinline__ unsigned cvtpk(float lo, float hi) {
  typedef float f32x2_ __attribute__((ext_vector_type(2)));
  typedef __bf16 bf16x2_ __attribute__((ext_vector_type(2)));
  f32x2_ v = {lo, hi};
  return __builtin_bit_cast(unsigned, __builtin_convertvector(v, bf16x2_));
}
__device__ __forceinline__ float rbf(float x) { return __builtin_bit_cast(float, cvtpk(x, x) << 16); }
__device__ __forceinline__ float bflo(unsigned u) { return __builtin_bit_cast(float, u << 16); }
__device__ __forceinline__ float bfhi(unsigned u) { return __builtin_bit_cast(float, u & 0xffff0000u); }
__device__ __forceinline__ float asf(unsigned u) { return __builtin_bit_cast(float, u); }
__device__ __forceinline__ unsigned asu(float f) { return __builtin_bit_cast(unsigned, f); }
__device__ __forceinline__ f32x4 mfma(v4u a, v4u b, f32x4 c) {
  return __builtin_amdgcn_mfma_f32_16x16x32_bf16(__builtin_bit_cast(bf16x8, a), __builtin_bit_cast(bf16x8, b), c, 0, 0, 0);
}
template <int CTRL> __device__ __forceinline__ float dpp(float x) {
  return __builtin_bit_cast(float, __builtin_amdgcn_mov_dpp(__builtin_bit_cast(int, x), CTRL, 0xf, 0xf, true));
}
constexpr int XOR1 = 0xB1, XOR2 = 0x4E, XOR7 = 0x141, ROR8 = 0x128, ROR4 = 0x124, NEWBC = 0x150;
__device__ __forceinline__ float x16_max(float x) {
  auto s = __builtin_amdgcn_permlane16_swap(asu(x), asu(x), false, false);
  return fmaxf(asf(s[0]), asf(s[1]));
}
__device__ __forceinline__ float x32_max(float x) {
  auto s = __builtin_amdgcn_permlane32_swap(asu(x), asu(x), false, false);
  return fmaxf(asf(s[0]), asf(s[1]));
}
__device__ __forceinline__ float x16_sum(float x) {
  auto s = __builtin_amdgcn_permlane16_swap(asu(x), asu(x), false, false);
  return asf(s[0]) + asf(s[1]);
}
__device__ __forceinline__ float x32_sum(float x) {
  auto s = __builtin_amdgcn_permlane32_swap(asu(x), asu(x), false, false);
  return asf(s[0]) + asf(s[1]);
}
__device__ __forceinline__ float wave_sum(float v) {
  v += dpp<XOR1>(v); v += dpp<XOR2>(v); v += dpp<XOR7>(v); v += dpp<ROR8>(v);
  v = x16_sum(v); v = x32_sum(v);
  return v;
}
__device__ __forceinline__ void lds_barrier() {
  asm volatile("s_waitcnt lgkmcnt(0)" ::: "memory");
  __builtin_amdgcn_s_barrier();
  asm volatile("" ::: "memory");
}
__device__ __forceinline__ void st_gran(unsigned char* ws, unsigned off, unsigned val, unsigned tag) {
  __hip_atomic_store((GAS unsigned long long*)(ws + off), ((unsigned long long)tag << 32) | val, RLX_AGENT);
}
#define UNPACK8(dst, o, v) do { dst[o + 0] = bflo(v.x); dst[o + 1] = bfhi(v.x); dst[o + 2] = bflo(v.y); dst[o + 3] = bfhi(v.y); \
  dst[o + 4] = bflo(v.z); dst[o + 5] = bfhi(v.z); dst[o + 6] = bflo(v.w); dst[o + 7] = bfhi(v.w); } while (0)
#define SB() __builtin_amdgcn_sched_barrier(0)

#define NSTAMP 13
#define STAMP(i) do { } while (0)
#define STAMP_FLUSH() do { } while (0)
#define WIN 6
#define NPOLL 5
#define FDELAY 24
#define PACE 4100
#define PACE0 300
#define PDEPTH 1
#define PSLEEP 1

struct LnStats { float mb, den; };
__device__ __forceinline__ LnStats ln_stats8(v4u v) {
  float xv[8]; UNPACK8(xv, 0, v);
  float s = ((xv[0] + xv[1]) + (xv[2] + xv[3])) + ((xv[4] + xv[5]) + (xv[6] + xv[7]));
  s = wave_sum(s);
  const float mean = s * (1.0f / D);
  float vs = 0.f;
#pragma unroll
  for (int i = 0; i < 8; ++i) { float d = xv[i] - mean; vs += d * d; }
  vs = wave_sum(vs);
  LnStats r;
  r.mb = rbf(mean);
  r.den = rbf(sqrtf(rbf(rbf(vs * (1.0f / D)) + 1e-5f)));
  return r;
}
template <int C> __device__ __forceinline__ v4u bop(unsigned pb, const v4u& mk) {
  int t = __builtin_amdgcn_update_dpp(0, (int)pb, NEWBC + C, 0xf, 0x3, false);
  t = __builtin_amdgcn_update_dpp(t, (int)pb, NEWBC + C + 1, 0xf, 0xC, false);
  const unsigned u = (unsigned)t;
  return v4u{u & mk.x, u & mk.y, u & mk.z, u & mk.w};
}
__device__ __forceinline__ float comb_rb(f32x4 acc, int lane) {
  const bool valid = (lane >> 5) == ((lane >> 3) & 1);
  float t[4];
#pragma unroll
  for (int r = 0; r < 4; ++r) { float u = valid ? acc[r] : 0.f; u += dpp<ROR8>(u); t[r] = x32_sum(u); }
  const float lo = (lane & 8) ? t[1] : t[0], hi = (lane & 8) ? t[3] : t[2];
  return (lane & 32) ? hi : lo;
}
__device__ __forceinline__ void comb2(f32x4 acc, int lane, float& va, float& vb) {
  float t[4];
#pragma unroll
  for (int r = 0; r < 4; ++r) {
    auto s = __builtin_amdgcn_permlane32_swap(asu(acc[r]), asu(acc[r]), false, false);
    t[r] = asf(s[0]) + dpp<ROR8>(asf(s[1]));
  }
  va = (lane & 32) ? t[2] : t[0];
  vb = (lane & 32) ? t[3] : t[1];
}
__device__ __forceinline__ float sum8(float v) { v += dpp<XOR1>(v); v += dpp<XOR2>(v); v += dpp<XOR7>(v); return v; }
__device__ __forceinline__ float max8(float v) { v = __builtin_amdgcn_fmed3f(v, dpp<XOR1>(v), INFINITY); v = __builtin_amdgcn_fmed3f(v, dpp<XOR2>(v), INFINITY); v = __builtin_amdgcn_fmed3f(v, dpp<XOR7>(v), INFINITY); return v; }
#define MEMFENCE() asm volatile("" ::: "memory")
#define PL_(r, o) "buffer_load_dwordx4 v[" r "], %[off], %[rs], 0 offen offset:" o " sc1\n"
__device__ __forceinline__ v2u poll128(__amdgpu_buffer_rsrc_t rs, int off, unsigned tag) {
  unsigned r0, r1, cnt;
  unsigned long long t;
  asm volatile(
      "s_nop 4\n s_mov_b32 %[cnt], 0\n"
      "LT%=:\n"
      PL_("248:251", "0")
      "s_waitcnt vmcnt(0)\n"
      "v_cmp_eq_u32 vcc, %[tag], v249\n v_cmp_eq_u32 %[t], %[tag], v251\n s_and_b64 vcc, vcc, %[t]\n"
      "s_cmp_eq_u64 vcc, exec\n s_cbranch_scc1 LA%=\n"
      "s_sleep %[sl]\n"
      "s_add_u32 %[cnt], %[cnt], 1\n s_cmp_lt_u32 %[cnt], %[cap]\n s_cbranch_scc1 LT%=\n"
      "LA%=:\n v_mov_b32 %[r0], v248\n v_mov_b32 %[r1], v250\n"
      : [r0] "=&v"(r0), [r1] "=&v"(r1), [cnt] "=&s"(cnt), [t] "=&s"(t)
      : [off] "v"(off), [rs] "s"(rs), [tag] "s"(tag), [sl] "n"(PSLEEP), [cap] "n"(SPIN_CAP)
      : "vcc", "scc", "memory", "v248", "v249", "v250", "v251");
  return v2u{r0, r1};
}
__device__ __forceinline__ v2u poll256(__amdgpu_buffer_rsrc_t rs, int off, unsigned tag, unsigned& r2, unsigned& r3) {
  unsigned r0, r1, cnt;
  unsigned long long t;
  asm volatile(
      "s_nop 4\n s_mov_b32 %[cnt], 0\n"
      "LT%=:\n"
      PL_("248:251", "0") PL_("252:255", "1024")
      "s_waitcnt vmcnt(0)\n"
      "v_cmp_eq_u32 vcc, %[tag], v249\n v_cmp_eq_u32 %[t], %[tag], v251\n s_and_b64 vcc, vcc, %[t]\n"
      "v_cmp_eq_u32 %[t], %[tag], v253\n s_and_b64 vcc, vcc, %[t]\n v_cmp_eq_u32 %[t], %[tag], v255\n s_and_b64 vcc, vcc, %[t]\n"
      "s_cmp_eq_u64 vcc, exec\n s_cbranch_scc1 LA%=\n"
      "s_sleep %[sl]\n"
      "s_add_u32 %[cnt], %[cnt], 1\n s_cmp_lt_u32 %[cnt], %[cap]\n s_cbranch_scc1 LT%=\n"
      "LA%=:\n v_mov_b32 %[r0], v248\n v_mov_b32 %[r1], v250\n v_mov_b32 %[r2], v252\n v_mov_b32 %[r3], v254\n"
      : [r0] "=&v"(r0), [r1] "=&v"(r1), [r2] "=&v"(r2), [r3] "=&v"(r3), [cnt] "=&s"(cnt), [t] "=&s"(t)
      : [off] "v"(off), [rs] "s"(rs), [tag] "s"(tag), [sl] "n"(PSLEEP), [cap] "n"(SPIN_CAP)
      : "vcc", "scc", "memory", "v248", "v249", "v250", "v251", "v252", "v253", "v254", "v255");
  return v2u{r0, r1};
}
__device__ __forceinline__ float vmax(float a, float b) { return __builtin_amdgcn_fmed3f(a, b, INFINITY); }
__device__ __forceinline__ v4u mv_masks(int lane) {
  const unsigned hm = (lane & 1) ? 0xffff0000u : 0x0000ffffu;
  const int dsel = (lane >> 1) & 3;
  return v4u{dsel == 0 ? hm : 0u, dsel == 1 ? hm : 0u, dsel == 2 ? hm : 0u, dsel == 3 ? hm : 0u};
}
template <bool NT> __device__ __forceinline__ v4u ldw(const GAS u16* p) {
  if constexpr (NT) return __builtin_nontemporal_load((const GAS v4u*)p); else return *(const GAS v4u*)p;
}

__global__ void __launch_bounds__(512, 2)
fused_kernel(const u16* __restrict__ x, const u16* __restrict__ kc, const u16* __restrict__ vc, const u16* __restrict__ wq,
             const u16* __restrict__ wo, const u16* __restrict__ wg, const u16* __restrict__ wu, const u16* __restrict__ wd,
             u16* __restrict__ out, unsigned char* ws) {
  extern __shared__ __attribute__((aligned(16))) unsigned char smem[];
  const int tid = threadIdx.x, lane = tid & 63, w = __builtin_amdgcn_readfirstlane(tid >> 6);
  LAS float* L_acc = (LAS float*)smem;
  LAS float* L_m = (LAS float*)(smem + 16384);
  LAS float* L_l = L_m + 32;
  LAS float* L_red = L_l + 32;
  LAS unsigned* L_q = (LAS unsigned*)(L_red + 128);
  LAS unsigned* L_h = L_q + 256;
  LAS float* L_mv = (LAS float*)(L_h + 256);
  LAS float* L_w2 = L_mv + 64 * 36;
  LAS float* L_r = L_w2 + 16;
  LAS float* L_st = L_r + 64;

  const int b = blockIdx.x, xcd = b & 7, r = b >> 3;
  const int g = r & 7, cls = r >> 3, s = xcd * 4 + cls;
  const int p = lane & 3, hd = (lane >> 2) & 3, a = lane >> 4;
  const int rb = (lane >> 3) & 1;
  const int xe = (7 - g) & 7;
  const bool q_role = cls == 0, g_role = cls == 1 && xcd != xe;
  STAMP(0);

  const __amdgpu_buffer_rsrc_t rws = __builtin_amdgcn_make_buffer_rsrc((void*)ws, 0, (int)WS_END, RSRC_W3);
  const __amdgpu_buffer_rsrc_t rk = __builtin_amdgcn_make_buffer_rsrc((void*)kc, 0, 8 * SK * 256, RSRC_W3);
  const __amdgpu_buffer_rsrc_t rv = __builtin_amdgcn_make_buffer_rsrc((void*)vc, 0, 8 * SK * 256, RSRC_W3);

  const unsigned epoch = *(const GAS unsigned*)(ws + WS_EPOCH);
  const float cshift = bflo(*(const GAS unsigned*)x);
  const int co = cls == 3 ? (r - 24) * 8 + xcd : 0;
  const v4u xq = *(const GAS v4u*)((const GAS unsigned*)x + 4 * co);
  const unsigned tstart = (unsigned)__builtin_amdgcn_s_memrealtime();
  int start, nblk;
  {
    const int par = (xcd & 1) ? SH_PAR : -SH_PAR;
    const int s1 = xcd == xe ? SH_X : SH_G;
    start = xcd * (SH_Q + SH_G + SH_2 + SH_3) + (xe < xcd ? SH_X - SH_G : 0) + ((xcd & 1) ? -4 * SH_PAR : 0);
    start += (cls > 0 ? SH_Q + par : 0) + (cls > 1 ? s1 + par : 0) + (cls > 2 ? SH_2 + par : 0);
    nblk = (cls == 0 ? SH_Q : cls == 1 ? s1 : cls == 2 ? SH_2 : SH_3) + par;
  }
  const int cnt = (nblk - w + 7) >> 3;
  const int kvoff = hd * 256 + a * 64 + p * 16;
  const int vvoff = lane * 16;
  const int blk0 = g * (SK * 256) + (start + w) * (TB * 1024);
  constexpr int BSTR = 8 * TB * 1024;
  v4u K[4][TB], V[4][TB];
#define ISSUE_K(slot, off) _Pragma("unroll") for (int t = 0; t < TB; ++t) K[slot][t] = __builtin_amdgcn_raw_buffer_load_b128(rk, kvoff, (off) + t * 1024, AUX_NT)
#define ISSUE_V(slot, off) _Pragma("unroll") for (int t = 0; t < TB; ++t) V[slot][t] = __builtin_amdgcn_raw_buffer_load_b128(rv, vvoff, (off) + t * 1024, AUX_NT)
  const unsigned tag = epoch + 1;
  if (q_role) {
    const v4u x8 = *(const GAS v4u*)((const GAS u16*)x + lane * 8);
    const unsigned xown = ((const GAS u16*)x)[tid];
    SB();
    v4u RW[8];
    const GAS u16* wp = (const GAS u16*)wq + (size_t)(64 * w + 16 * a + rb) * 4096 + g * 512 + xcd * 64 + (lane & 7) * 8;
#pragma unroll
    for (int i = 0; i < 8; ++i) RW[i] = ldw<WAUX == 2>(wp + (size_t)(2 * i) * 4096);
    SB();
    const v4u mk = mv_masks(lane);
    const LnStats st = ln_stats8(x8);
    const float xn = rbf(rbf(bflo(xown) - st.mb) / st.den);
    const unsigned pb = cvtpk(xn, xn);
    f32x4 acc = {0, 0, 0, 0};
    acc = mfma(RW[0], bop<0>(pb, mk), acc); acc = mfma(RW[1], bop<2>(pb, mk), acc);
    acc = mfma(RW[2], bop<4>(pb, mk), acc); acc = mfma(RW[3], bop<6>(pb, mk), acc);
    acc = mfma(RW[4], bop<8>(pb, mk), acc); acc = mfma(RW[5], bop<10>(pb, mk), acc);
    acc = mfma(RW[6], bop<12>(pb, mk), acc); acc = mfma(RW[7], bop<14>(pb, mk), acc);
    if ((lane >> 5) == rb) {
#pragma unroll
      for (int i = 0; i < 4; ++i) L_mv[(8 * (4 * (a & 1) + i) + (lane & 7)) * 20 + 2 * w + rb] = acc[i];
    }
    lds_barrier();
    if (w == 0) {
      const f32x4 r0 = *(LAS f32x4*)(L_mv + lane * 20), r1 = *(LAS f32x4*)(L_mv + lane * 20 + 4);
      const f32x4 r2 = *(LAS f32x4*)(L_mv + lane * 20 + 8), r3 = *(LAS f32x4*)(L_mv + lane * 20 + 12);
      const f32x4 t = (r0 + r1) + (r2 + r3);
      const float q = (t[0] + t[1]) + (t[2] + t[3]);
      const float qn = dpp<XOR1>(q);
      if ((lane & 1) == 0) st_gran(ws, WS_QG + (g * 256 + xcd * 32 + (lane >> 1)) * 8, cvtpk(q, qn), tag);
    }
    STAMP(1);
    SB();
  }
  v4u qf;
  {
    const int qoff = (w == 0 ? 0 : OOB) + WS_QG + (g * 256 + hd * 64 + 4 * (4 * a + p)) * 8;
    v4u q0 = {0, 0, 0, 0}, q1 = {0, 0, 0, 0};
    v4u P[NPOLL][2];
    bool ok = false;
#pragma unroll
    for (int j = 0; j < 42; ++j) {
      const int jj = j < 8 ? j : j - 8;
      const int un = j < 8 ? (j >> 2) : 2 + jj / 6, in = j < 8 ? (j & 3) : jj % 6;
      if (j >= WIN) __builtin_amdgcn_s_waitcnt(0x0F70 | (WIN - 1));
      if (j >= 12 + 1 + WIN && (j - (13 + WIN)) % 6 == 0) {
        const int pj = (j - (13 + WIN)) / 6;
        asm volatile("" : "+v"(P[pj][0]), "+v"(P[pj][1]) :: "memory");
        const bool okj = __all(P[pj][0].y == tag && P[pj][0].w == tag && P[pj][1].y == tag && P[pj][1].w == tag);
        if (!ok) { q0 = P[pj][0]; q1 = P[pj][1]; }
        ok = ok || okj;
      }
      SB();
      if (in < 4) {
        const int sl = un >> 1;
        if (un & 1) V[sl][in] = __builtin_amdgcn_raw_buffer_load_b128(rv, vvoff, blk0 + sl * BSTR + in * 1024, AUX_NT);
        else        K[sl][in] = __builtin_amdgcn_raw_buffer_load_b128(rk, kvoff, blk0 + sl * BSTR + in * 1024, AUX_NT);
      } else {
        P[un - 2][in - 4] = __builtin_amdgcn_raw_buffer_load_b128(rws, qoff + (in - 4) * 16, 0, AUX_SC1);
      }
      SB();
    }
#pragma unroll
    for (int pj = 0; pj < NPOLL; ++pj) {
      if (13 + WIN + 6 * pj >= 42) {
        asm volatile("" : "+v"(P[pj][0]), "+v"(P[pj][1]) :: "memory");
        const bool okj = __all(P[pj][0].y == tag && P[pj][0].w == tag && P[pj][1].y == tag && P[pj][1].w == tag);
        if (!ok) { q0 = P[pj][0]; q1 = P[pj][1]; }
        ok = ok || okj;
      }
    }
    SB();
    if (w == 0) {
      if (!ok) {
        unsigned spins = 0;
        for (;;) {
          asm volatile("" ::: "memory");
          q0 = __builtin_amdgcn_raw_buffer_load_b128(rws, qoff, 0, AUX_SC1);
          q1 = __builtin_amdgcn_raw_buffer_load_b128(rws, qoff + 16, 0, AUX_SC1);
          if (__all(q0.y == tag && q0.w == tag && q1.y == tag && q1.w == tag)) break;
          if (++spins > SPIN_CAP) break;
        }
      }
      *(LAS v4u*)(L_q + lane * 4) = v4u{q0.x, q0.z, q1.x, q1.z};
    }
    lds_barrier();
    qf = *(LAS v4u*)(L_q + lane * 4);
  }
  STAMP(2);
  v4u WO[4];
  const int gj = xcd * 7 + (g < 7 - xcd ? g : g - 1);
  v4u GW[8], WD[8];
  const __amdgpu_buffer_rsrc_t rg = __builtin_amdgcn_make_buffer_rsrc((void*)wg, 0, D * FF * 2, RSRC_W3);
  const __amdgpu_buffer_rsrc_t ru = __builtin_amdgcn_make_buffer_rsrc((void*)wu, 0, D * FF * 2, RSRC_W3);
  const __amdgpu_buffer_rsrc_t rd = __builtin_amdgcn_make_buffer_rsrc((void*)wd, 0, D * FF * 2, RSRC_W3);
  const __amdgpu_buffer_rsrc_t ro = __builtin_amdgcn_make_buffer_rsrc((void*)wo, 0, 4096 * D * 2, RSRC_W3);
#define LOAD_GU() do { \
    const int vo_ = (16 * a + 4 * hd) * (FF * 2) + p * 16; \
    const int so_ = g_role ? 64 * w * (FF * 2) + 64 * gj : OOB; \
    _Pragma("unroll") for (int i = 0; i < 4; ++i) GW[i] = __builtin_amdgcn_raw_buffer_load_b128(rg, vo_, so_ + i * (FF * 2), WAUX); \
    _Pragma("unroll") for (int i = 0; i < 4; ++i) GW[4 + i] = __builtin_amdgcn_raw_buffer_load_b128(ru, vo_, so_ + i * (FF * 2), WAUX); \
    SB(); } while (0)
#define LOAD_WO() do { \
    const int vo_ = (2 * a + rb) * (D * 2) + (lane & 7) * 16; \
    const int so_ = w < 4 ? (512 * g + 16 * s) * (D * 2) + 256 * w : OOB; \
    _Pragma("unroll") for (int i = 0; i < 4; ++i) WO[i] = __builtin_amdgcn_raw_buffer_load_b128(ro, vo_, so_ + (i & 1) * 8 * (D * 2) + (i >> 1) * 128, WAUX); \
    SB(); } while (0)

  constexpr unsigned pstep = (unsigned)(PACE * 16) / (unsigned)SH_3;
  const unsigned pbase = tstart + (((w + 1) * pstep) >> 4) - PACE0;
  const unsigned maskA = p == 0 ? 0x0000ffffu : (p == 1 ? 0xffff0000u : 0u);
  const unsigned maskB = p == 2 ? 0x0000ffffu : (p == 3 ? 0xffff0000u : 0u);
  constexpr float SC = 0.08838834764831845f, L2E = 1.4426950408889634f;
  float m = -INFINITY, l = 0.f;
  f32x4 acc0 = {0, 0, 0, 0}, acc1 = {0, 0, 0, 0};
#define BLOCK(k) do { \
    constexpr int slot = (k) & 3; \
    float sc[TB]; \
    _Pragma("unroll") for (int t = 0; t < TB; ++t) { \
      f32x4 d = mfma(K[slot][t], qf, f32x4{0, 0, 0, 0}); \
      float xs = dpp<0x00>(d[0]) + dpp<0x55>(d[1]) + dpp<0xAA>(d[2]) + dpp<0xFF>(d[3]); \
      sc[t] = rbf(rbf(xs) * SC); \
    } \
    const int noff = ((k) + 4 < NBMIN || (k) + 4 < cnt) ? blk0 + ((k) + 4) * BSTR : OOB; \
    if (PACE > 0 && (k) + 4 >= 6 && (k) + 4 < NBMAX && ((k) + 4 < NBMIN || (k) + 4 < cnt)) { \
      const unsigned tgt_ = pbase + ((((k) + 4) * 8 * pstep) >> 4); \
      while ((int)((unsigned)__builtin_amdgcn_s_memrealtime() - tgt_) < 0) __builtin_amdgcn_s_sleep(2); \
    } \
    SB(); \
    if ((k) + 4 < NBMAX) { ISSUE_K(slot, noff); } \
    SB(); \
    float bm = fmaxf(fmaxf(sc[0], sc[1]), fmaxf(sc[2], sc[3])); \
    bm = x16_max(bm); bm = x32_max(bm); \
    const float mn = fmaxf(m, bm); \
    const float alpha = __builtin_amdgcn_exp2f((m - mn) * L2E); \
    m = mn; \
    l *= alpha; \
    acc0 *= alpha; acc1 *= alpha; \
    _Pragma("unroll") for (int t = 0; t < TB; ++t) { \
      const float pe = __builtin_amdgcn_exp2f((sc[t] - mn) * L2E); \
      const unsigned pb = cvtpk(pe, pe); \
      l += bflo(pb); \
      const v4u A1 = {pb & maskA, pb & maskB, 0u, 0u}; \
      const v4u A2 = {0u, 0u, pb & maskA, pb & maskB}; \
      acc0 = mfma(V[slot][t], A1, acc0); \
      acc1 = mfma(V[slot][t], A2, acc1); \
    } \
    SB(); \
    if ((k) + 4 < NBMAX) { ISSUE_V(slot, noff); } \
    SB(); \
  } while (0)
#define SETPRIO(k) do { if ((k) == 0) __builtin_amdgcn_s_setprio(3); if ((k) == NBMIN - 8) __builtin_amdgcn_s_setprio(2); \
    if ((k) == NBMIN - 4) __builtin_amdgcn_s_setprio(1); if ((k) == NBMIN - 2) __builtin_amdgcn_s_setprio(0); } while (0)
  constexpr int KGU = cmin(NBMAX - 4, NBMIN - 1), KWD = cmin(NBMAX - 3, NBMIN - 1);
#define BLK(k) do { if constexpr ((k) < NBMIN) { SETPRIO(k); BLOCK(k); if constexpr ((k) == KGU) LOAD_GU(); if constexpr ((k) == KWD) LOAD_WO(); } } while (0)
  BLK(0); BLK(1); BLK(2); BLK(3); BLK(4); BLK(5); BLK(6); BLK(7); BLK(8); BLK(9); BLK(10); BLK(11); BLK(12); BLK(13); BLK(14); BLK(15);
  static_assert(NBMIN <= 16 && NBMAX - NBMIN <= 4, "share range");
  if constexpr (NBMAX > NBMIN) {
    if (cnt > NBMIN) {
      BLOCK(NBMIN);
      if constexpr (NBMAX > NBMIN + 1) {
        if (cnt > NBMIN + 1) {
          BLOCK(NBMIN + 1);
          if constexpr (NBMAX > NBMIN + 2) {
            if (cnt > NBMIN + 2) {
              BLOCK(NBMIN + 2);
              if constexpr (NBMAX > NBMIN + 3) { if (cnt > NBMIN + 3) { BLOCK(NBMIN + 3); } }
            }
          }
        }
      }
    }
  }
#pragma unroll
  for (int kk = NBMIN; kk < NBMAX; ++kk) {
#pragma unroll
    for (int t = 0; t < TB; ++t) asm volatile("" :: "v"(K[kk & 3][t]), "v"(V[kk & 3][t]));
  }
  STAMP(3);
  int lane2 = lane;
  asm volatile("" : "+v"(lane2));
  const v4u mk = mv_masks(lane2);
  l = x16_sum(l); l = x32_sum(l);
  *(LAS f32x4*)(L_acc + (w * 64 + lane) * 8) = acc0;
  *(LAS f32x4*)(L_acc + (w * 64 + lane) * 8 + 4) = acc1;
  if (lane < 16 && p == 0) { L_m[hd * 8 + w] = m; L_l[hd * 8 + w] = l; }
  lds_barrier();
  {
    const int h = tid >> 7, d = tid & 127;
    const f32x4 m0 = *(LAS f32x4*)(L_m + h * 8), m1 = *(LAS f32x4*)(L_m + h * 8 + 4);
    const float M = vmax(vmax(vmax(m0[0], m0[1]), vmax(m0[2], m0[3])), vmax(vmax(m1[0], m1[1]), vmax(m1[2], m1[3])));
    float wt[8];
#pragma unroll
    for (int i = 0; i < 4; ++i) { wt[i] = __builtin_amdgcn_exp2f((m0[i] - M) * L2E); wt[4 + i] = __builtin_amdgcn_exp2f((m1[i] - M) * L2E); }
    const int idx = (16 * (d >> 5) + 4 * h + (d & 3)) * 8 + ((d >> 2) & 1) * 4 + ((d >> 3) & 3);
    float oe = wt[0] * L_acc[idx], oo = wt[1] * L_acc[512 + idx];
#pragma unroll
    for (int ww = 2; ww < 8; ww += 2) { oe += wt[ww] * L_acc[ww * 512 + idx]; oo += wt[ww + 1] * L_acc[(ww + 1) * 512 + idx]; }
    const f32x4 l0 = *(LAS f32x4*)(L_l + h * 8), l1 = *(LAS f32x4*)(L_l + h * 8 + 4);
    const float L = ((wt[0] * l0[0] + wt[1] * l0[1]) + (wt[2] * l0[2] + wt[3] * l0[3])) + ((wt[4] * l1[0] + wt[5] * l1[1]) + (wt[6] * l1[2] + wt[7] * l1[3]));
    const int sc_ = tid >> 4, v = tid & 15;
    if (d < 2) st_gran(ws, WS_ML + (((g * 4 + h) * 32 + s) * 2 + d) * 8, asu(d ? L : M), tag);
    st_gran(ws, WS_PO + (((g * 32 + sc_) * 32 + s) * 16 + v) * 8, asu(oe + oo), tag);
  }
  STAMP(4);
  {
    const int h = g * 4 + (s >> 3);
    const int pr = 4 * w + ((lane >> 3) & 3);
    const int ooff = lane < 32 ? WS_PO + ((g * 32 + s) * 512 + pr * 16 + 2 * (lane & 7)) * 8 : WS_ML + (h * 32 + pr) * 16;
    const v2u pv_ = poll128(rws, ooff, tag);
    STAMP(5);
    auto sx = __builtin_amdgcn_permlane32_swap(pv_.x, pv_.x, false, false);
    auto sz = __builtin_amdgcn_permlane32_swap(pv_.y, pv_.y, false, false);
    const float Ms = asf(sx[1]);
    const float Mw = x16_max(fmaxf(Ms, dpp<ROR8>(Ms)));
    const float wt = __builtin_amdgcn_exp2f((Ms - Mw) * L2E);
    float o0 = wt * asf(sx[0]), o1 = wt * asf(sz[0]), lw = wt * asf(sz[1]);
    o0 += dpp<ROR8>(o0); o1 += dpp<ROR8>(o1); lw += dpp<ROR8>(lw);
    o0 = x16_sum(o0); o1 = x16_sum(o1); lw = x16_sum(lw);
    if (lane < 8) { L_red[lane * 16 + w] = o0; L_red[lane * 16 + 8 + w] = o1; }
    if (lane == 0) { L_w2[w] = Mw; L_w2[8 + w] = lw; }
    lds_barrier();
    if (w < 4) {
      const float pa_ = L_red[lane], pb_ = L_red[64 + lane];
      const float Mv = L_w2[lane & 7], Lv = L_w2[8 + (lane & 7)];
      const float M = max8(Mv);
      const float f = __builtin_amdgcn_exp2f((Mv - M) * L2E);
      const float ta = sum8(f * pa_), tb = sum8(f * pb_), tl = sum8(f * Lv);
      const float rl = __builtin_amdgcn_rcpf(tl);
      const float oa = ta * rl, ob = tb * rl;
      const unsigned pa = cvtpk(oa, oa), pb = cvtpk(ob, ob);
      const v4u Ba = {pa & mk.x, pa & mk.y, pa & mk.z, pa & mk.w}, Bb = {pb & mk.x, pb & mk.y, pb & mk.z, pb & mk.w};
      f32x4 c0 = {0, 0, 0, 0}, c1 = c0;
      c0 = mfma(WO[0], Ba, c0); c1 = mfma(WO[2], Ba, c1);
      c0 = mfma(WO[1], Bb, c0); c1 = mfma(WO[3], Bb, c1);
      float va, vb, vc_, vd;
      comb2(c0, lane, va, vb);
      comb2(c1, lane, vc_, vd);
      const int col = 128 * w + 8 * (4 * (a & 1) + 2 * (lane >> 5)) + (lane & 7);
      if (rb == 0) {
        const unsigned o_ = WS_HP + (((col >> 3) * 256 + b) * 8 + (col & 7)) * 8;
        st_gran(ws, o_, asu(va), tag);
        st_gran(ws, o_ + 16384, asu(vb), tag);
        st_gran(ws, o_ + 8 * 16384, asu(vc_), tag);
        st_gran(ws, o_ + 9 * 16384, asu(vd), tag);
      }
    }
  }
  STAMP(6);
  if (cls == 3) {
    unsigned r2, r3;
    const v2u hp = poll256(rws, WS_HP + co * 16384 + w * 2048 + lane * 16, tag, r2, r3);
    STAMP(7);
    float s0 = asf(hp.x) + asf(r2), s1 = asf(hp.y) + asf(r3);
    s0 += dpp<ROR4>(s0); s1 += dpp<ROR4>(s1); s0 += dpp<ROR8>(s0); s1 += dpp<ROR8>(s1);
    s0 = x16_sum(s0); s1 = x16_sum(s1); s0 = x32_sum(s0); s1 = x32_sum(s1);
    if (lane < 4) { L_r[w * 8 + 2 * lane] = s0; L_r[w * 8 + 2 * lane + 1] = s1; }
    lds_barrier();
    if (w == 1) {
      const float t = sum8(L_r[(lane & 7) * 8 + (lane >> 3)]);
      const float tn = dpp<ROR8>(t);
      const unsigned xp = a == 0 ? xq.x : a == 1 ? xq.y : a == 2 ? xq.z : xq.w;
      const float h0 = bflo(xp) + rbf(t), h1 = bfhi(xp) + rbf(tn);
      if ((lane & 15) == 0) st_gran(ws, WS_HG + (4 * co + a) * 8, cvtpk(h0, h1), tag);
    }
    STAMP(8);
  }
  if (g_role) {
    {
      const int vo_ = (16 * (a & 1) + 8 * (a >> 1) + rb) * (D * 2) + (lane & 7) * 16;
      const int so_ = w < 4 ? 32 * gj * (D * 2) + 256 * w : OOB;
#pragma unroll
      for (int i = 0; i < 8; ++i) WD[i] = __builtin_amdgcn_raw_buffer_load_b128(rd, vo_, so_ + 2 * (i & 3) * (D * 2) + (i >> 2) * 128, WAUX);
      SB();
    }
    float cs;
    {
      const unsigned ta = 0x3F803F80u & maskA, tb = 0x3F803F80u & maskB;
      const v4u B1 = {ta, tb, 0u, 0u}, B2 = {0u, 0u, ta, tb};
      f32x4 g0 = {0, 0, 0, 0}, g1 = g0, u0 = g0, u1 = g0;
#pragma unroll
      for (int i = 0; i < 4; ++i) { g0 = mfma(GW[i], B1, g0); g1 = mfma(GW[i], B2, g1); u0 = mfma(GW[4 + i], B1, u0); u1 = mfma(GW[4 + i], B2, u1); }
      if (a == hd) {
#pragma unroll
        for (int i = 0; i < 4; ++i) {
          LAS float* q_ = L_mv + (8 * i + p) * 36 + 4 * w + a;
          q_[0] = g0[i]; q_[4 * 36] = g1[i]; q_[32 * 36] = u0[i]; q_[36 * 36] = u1[i];
        }
      }
      lds_barrier();
      f32x4 t = *(LAS f32x4*)(L_mv + lane * 36);
#pragma unroll
      for (int i = 1; i < 8; ++i) t += *(LAS f32x4*)(L_mv + lane * 36 + 4 * i);
      cs = (t[0] + t[1]) + (t[2] + t[3]);
      lds_barrier();
    }
    const v2u hg = poll128(rws, WS_HG + (32 * w + 8 * a + 2 * hd) * 8, tag);
    STAMP(9);
    {
      const unsigned t4[4] = {__builtin_amdgcn_perm(hg.x, hg.x, 0x01000100u), __builtin_amdgcn_perm(hg.x, hg.x, 0x03020302u),
                              __builtin_amdgcn_perm(hg.y, hg.y, 0x01000100u), __builtin_amdgcn_perm(hg.y, hg.y, 0x03020302u)};
      f32x4 g0 = {0, 0, 0, 0}, g1 = g0, u0 = g0, u1 = g0;
#pragma unroll
      for (int i = 0; i < 4; ++i) {
        const unsigned ta = t4[i] & maskA, tb = t4[i] & maskB;
        const v4u B1 = {ta, tb, 0u, 0u}, B2 = {0u, 0u, ta, tb};
        g0 = mfma(GW[i], B1, g0); g1 = mfma(GW[i], B2, g1); u0 = mfma(GW[4 + i], B1, u0); u1 = mfma(GW[4 + i], B2, u1);
      }
      const float d0 = bflo(hg.x) - cshift, d1 = bfhi(hg.x) - cshift, d2 = bflo(hg.y) - cshift, d3 = bfhi(hg.y) - cshift;
      float s1 = (d0 + d1) + (d2 + d3), s2 = (d0 * d0 + d1 * d1) + (d2 * d2 + d3 * d3);
      s1 += dpp<ROR4>(s1); s2 += dpp<ROR4>(s2); s1 += dpp<ROR8>(s1); s2 += dpp<ROR8>(s2);
      s1 = x16_sum(s1); s2 = x16_sum(s2); s1 = x32_sum(s1); s2 = x32_sum(s2);
      if (lane == 0) { L_st[2 * w] = s1; L_st[2 * w + 1] = s2; }
      if (a == hd) {
#pragma unroll
        for (int i = 0; i < 4; ++i) {
          LAS float* q_ = L_mv + (8 * i + p) * 36 + 4 * w + a;
          q_[0] = g0[i]; q_[4 * 36] = g1[i]; q_[32 * 36] = u0[i]; q_[36 * 36] = u1[i];
        }
      }
    }
    lds_barrier();
    if (w < 4) {
      f32x4 r_[8];
#pragma unroll
      for (int i = 0; i < 8; ++i) r_[i] = *(LAS f32x4*)(L_mv + lane * 36 + 4 * i);
      const float s1 = sum8(L_st[2 * (lane & 7)]) * (1.0f / D), s2 = sum8(L_st[2 * (lane & 7) + 1]) * (1.0f / D);
      const float mb = rbf(cshift + s1);
      const float var = vmax(s2 - s1 * s1, 0.f);
      const float den = rbf(__builtin_amdgcn_sqrtf(rbf(rbf(var) + 1e-5f)));
      const float rdn = __builtin_amdgcn_rcpf(den);
      const f32x4 t = ((r_[0] + r_[1]) + (r_[2] + r_[3])) + ((r_[4] + r_[5]) + (r_[6] + r_[7]));
      const float tot = rbf((((t[0] + t[1]) + (t[2] + t[3])) - mb * cs) * rdn);
      auto sw = __builtin_amdgcn_permlane32_swap(asu(tot), asu(tot), false, false);
      const float gt = asf(sw[0]), up = asf(sw[1]);
      const float sg = rbf(__builtin_amdgcn_rcpf(1.0f + __builtin_amdgcn_exp2f(-gt * L2E)));
      const float hh = sg * up;
      unsigned ph = cvtpk(hh, hh);
      ph = (unsigned)__builtin_amdgcn_update_dpp((int)ph, (int)ph, ROR8, 0xC, 0xf, false);
      const v4u B0 = bop<0>(ph, mk), B1 = bop<2>(ph, mk), B2 = bop<4>(ph, mk), B3 = bop<6>(ph, mk);
      f32x4 c0 = {0, 0, 0, 0}, c1 = c0, c2 = c0, c3 = c0;
      c0 = mfma(WD[0], B0, c0); c1 = mfma(WD[4], B0, c1); c2 = mfma(WD[1], B1, c2); c3 = mfma(WD[5], B1, c3);
      c0 = mfma(WD[2], B2, c0); c1 = mfma(WD[6], B2, c1); c2 = mfma(WD[3], B3, c2); c3 = mfma(WD[7], B3, c3);
      c0 += c2; c1 += c3;
      float va, vb, vc_, vd;
      comb2(c0, lane, va, vb);
      comb2(c1, lane, vc_, vd);
      const int col = 128 * w + 8 * (4 * (a & 1) + 2 * (lane >> 5)) + (lane & 7);
      if (rb == 0) {
        const unsigned o_ = WS_OP + (((col >> 1) * 64 + gj) * 2 + (col & 1)) * 8;
        st_gran(ws, o_, asu(va), tag);
        st_gran(ws, o_ + 4 * 1024, asu(vb), tag);
        st_gran(ws, o_ + 32 * 1024, asu(vc_), tag);
        st_gran(ws, o_ + 36 * 1024, asu(vd), tag);
      }
    }
    STAMP(10);
  }
  if (w == 6) {
    v2u hg;
    unsigned spins = 0;
    for (;;) {
      MEMFENCE();
      hg = __builtin_amdgcn_raw_buffer_load_b64(rws, WS_HG + b * 8, 0, AUX_SC1);
      if (__all(hg.y == tag)) break;
      if (++spins > SPIN_CAP) break;
      __builtin_amdgcn_s_sleep(4);
    }
    __builtin_amdgcn_s_sleep(FDELAY);
    const v2u op = poll128(rws, WS_OP + (b * 64 + (lane < 56 ? lane : 0)) * 16, tag);
    STAMP(11);
    float f0 = lane < 56 ? asf(op.x) : 0.f, f1 = lane < 56 ? asf(op.y) : 0.f;
    f0 += dpp<XOR1>(f0); f1 += dpp<XOR1>(f1); f0 += dpp<XOR2>(f0); f1 += dpp<XOR2>(f1);
    f0 += dpp<XOR7>(f0); f1 += dpp<XOR7>(f1); f0 += dpp<ROR8>(f0); f1 += dpp<ROR8>(f1);
    f0 = x16_sum(f0); f1 = x16_sum(f1); f0 = x32_sum(f0); f1 = x32_sum(f1);
    const float o0 = bflo(hg.x) + rbf(f0), o1 = bfhi(hg.x) + rbf(f1);
    if (lane == 0) {
      ((GAS unsigned*)out)[b] = cvtpk(o0, o1);
      if (b == 0) __hip_atomic_store((GAS unsigned*)(ws + WS_EPOCH), tag, RLX_AGENT);
    }
    STAMP(12);
  }
  STAMP_FLUSH();
}

extern "C" void kernel_launch(void* const* d_in, const int* in_sizes, int n_in, void* d_out, int out_size, void* d_ws, size_t ws_size,
                              hipStream_t stream) {
  fused_kernel<<<256, 512, 32 * 1024, stream>>>((const u16*)d_in[0], (const u16*)d_in[1], (const u16*)d_in[2], (const u16*)d_in[3],
                                                (const u16*)d_in[4], (const u16*)d_in[6], (const u16*)d_in[7], (const u16*)d_in[8],
                                                (u16*)d_out, (unsigned char*)d_ws);
}
```
